```python
import torch

def _softmax(x, axis=-1):
    return torch.softmax(x, dim=axis)
def _gelu(x):
    return torch.nn.functional.gelu(x, approximate="tanh")
def _silu(x):
    return torch.nn.functional.silu(x)
def _sigmoid(x):
    return torch.sigmoid(x)
def _layernorm(x, axis=-1, eps=1e-5):
    m = x.mean(dim=axis, keepdim=True)
    v = x.var(dim=axis, keepdim=True, unbiased=False)
    return (x - m) / torch.sqrt(v + eps)
def _scatter_add(tgt, idx, src, axis=0):
    return tgt.clone().index_add_(axis, idx, src)
def _segment_sum(src, seg, n):
    out = torch.zeros((n,) + src.shape[1:], dtype=src.dtype)
    return out.index_add_(0, seg, src)
def _pad(x, pw):
    flat = tuple(v for pair in reversed(pw) for v in pair)
    return torch.nn.functional.pad(x, flat)

kernel_name = 'parallel_sum__sliding_block_cached__mixer_channel_mlp__decode'

def setup_inputs():

    _w = (lambda a: (lambda t: t.to(torch.bfloat16) if t.is_floating_point() else t)(torch.as_tensor(a)))
    def _A_setup():
        B, Sq, D = 1, 1, 640
        Hq, Hkv, Sk, Dh = 32, 32, 4096, 128
        assert Hq % Hkv == 0, (Hq, Hkv)
        return {
            "x": _w((lambda *s: torch.randn(*s, dtype=torch.bfloat16) * 0.05)(B, Sq, D)),
            "k_cache": _w((lambda *s: torch.randn(*s, dtype=torch.bfloat16) * 3.2)(B, Hkv, Sk, Dh)),
            "v_cache": _w((lambda *s: torch.randn(*s, dtype=torch.bfloat16) * 0.4)(B, Hkv, Sk, Dh)),
            "wq": _w((lambda *s: torch.randn(*s, dtype=torch.bfloat16) * 3.2)(D, Hq * Dh) * (D ** -0.5)),
            "wo": _w((lambda *s: torch.randn(*s, dtype=torch.bfloat16) * 0.4)(Hq * Dh, D) * ((Hq * Dh) ** -0.5)),
        }
    _A_in = _A_setup()

    def _B_setup():
        B, S, D, F = 1, 1, 640, 1728
        return {
            "x": _w((lambda *s: torch.randn(*s, dtype=torch.bfloat16) * 0.05)(B, S, D)),
            "w1": _w((lambda *s: torch.randn(*s, dtype=torch.bfloat16) * 3.2)(D, F) * (D ** -0.5)),
            "b1": _w((lambda *s: torch.randn(*s, dtype=torch.bfloat16) * 0.8)(F)),
            "w2": _w((lambda *s: torch.randn(*s, dtype=torch.bfloat16) * 0.1)(F, D) * (F ** -0.5)),
            "b2": _w((lambda *s: torch.randn(*s, dtype=torch.bfloat16) * 0.1)(D)),
        }
    _B_in = _B_setup()

    return {
        "A_x": _A_in["x"],
        "A_k_cache": _A_in["k_cache"],
        "A_v_cache": _A_in["v_cache"],
        "A_wq": _A_in["wq"],
        "A_wo": _A_in["wo"],
        "B_x": _B_in["x"],
        "B_w1": _B_in["w1"],
        "B_b1": _B_in["b1"],
        "B_w2": _B_in["w2"],
        "B_b2": _B_in["b2"],
    }

def reference(A_x, A_k_cache, A_v_cache, A_wq, A_wo, B_x, B_w1, B_b1, B_w2, B_b2):
    def _A_ref(x, k_cache, v_cache, wq, wo):
        B, Sq, D = x.shape
        Hq, Dh = 32, 128
        Hkv, Sk = k_cache.shape[1], k_cache.shape[2]
        W = min(4096, max(256, Sk // 4))
        rep = Hq // Hkv
        q = (lambda x, *d: x.permute(*d))((x @ wq).reshape(B, Sq, Hq, Dh), 0, 2, 1, 3)
        k = (lambda x, n, axis: torch.repeat_interleave(x, n, dim=axis))(k_cache[:, :, Sk - W:, :], rep, axis=1)
        v = (lambda x, n, axis: torch.repeat_interleave(x, n, dim=axis))(v_cache[:, :, Sk - W:, :], rep, axis=1)
        scores = torch.einsum("bhqd,bhkd->bhqk", q, k) * (Dh ** -0.5)
        p = _softmax(scores, axis=-1)
        o = torch.einsum("bhqk,bhkd->bhqd", p, v)
        return (lambda x, *d: x.permute(*d))(o, 0, 2, 1, 3).reshape(B, Sq, Hq * Dh) @ wo

    def _B_ref(x, w1, b1, w2, b2):
        y = _layernorm(x, axis=-1)
        h = _gelu(torch.einsum("bsd,df->bsf", y, w1) + b1)
        return torch.einsum("bsf,fd->bsd", h, w2) + b2

    x = A_x
    xn = _layernorm(x, axis=-1)
    return x + _A_ref(xn, A_k_cache, A_v_cache, A_wq, A_wo) + _B_ref(xn, B_w1, B_b1, B_w2, B_b2)
```

```cpp
#include <hip/hip_runtime.h>
#include <hip/hip_bf16.h>

#define GAS __attribute__((address_space(1)))
typedef unsigned v4u __attribute__((ext_vector_type(4)));
typedef GAS unsigned gu32;
typedef GAS unsigned long long gu64;
#define RLX_AGENT __ATOMIC_RELAXED, __HIP_MEMORY_SCOPE_AGENT

constexpr int D = 640, H = 32, DH = 128, SK = 4096, WIN = 1024, F = 1728;
constexpr int X1N = 192, X2N = 130, X3N = 160;
constexpr int OFF_X1 = 32;
constexpr int OFF_X2 = OFF_X1 + 256 * X1N;
constexpr int OFF_X3 = OFF_X2 + 256 * 136;
constexpr unsigned SPIN_CAP = 1u << 22;

__device__ __forceinline__ float blo(unsigned u) { return __uint_as_float(u << 16); }
__device__ __forceinline__ float bhi(unsigned u) { return __uint_as_float(u & 0xffff0000u); }
__device__ __forceinline__ float rbf(float f) { return __bfloat162float(__float2bfloat16(f)); }
__device__ __forceinline__ void unpack8(const v4u& u, float (&f)[8]) {
    f[0] = blo(u.x); f[1] = bhi(u.x); f[2] = blo(u.y); f[3] = bhi(u.y);
    f[4] = blo(u.z); f[5] = bhi(u.z); f[6] = blo(u.w); f[7] = bhi(u.w);
}
__device__ __forceinline__ float wsum(float v) {
#pragma unroll
    for (int o = 32; o; o >>= 1) v += __shfl_xor(v, o);
    return v;
}
__device__ __forceinline__ void put(gu64* g, unsigned tag, float v) {
    __hip_atomic_store(g, ((unsigned long long)tag << 32) | __float_as_uint(v), RLX_AGENT);
}
template <int N>
__device__ __forceinline__ void sweep(gu64* g, int S, unsigned tag, float (&v)[N]) {
    for (unsigned spins = 0; spins < SPIN_CAP; ++spins) {
        bool ok = true;
#pragma unroll
        for (int k = 0; k < N; ++k) {
            const unsigned long long q = __hip_atomic_load(g + (size_t)k * S, RLX_AGENT);
            v[k] = __uint_as_float((unsigned)q);
            ok &= (unsigned)(q >> 32) == tag;
        }
        if (ok) break;
    }
}

__global__ __launch_bounds__(256) void fused_kernel(
    const __hip_bfloat16* __restrict__ x, const __hip_bfloat16* __restrict__ kc,
    const __hip_bfloat16* __restrict__ vc, const __hip_bfloat16* __restrict__ wq,
    const __hip_bfloat16* __restrict__ wo, const __hip_bfloat16* __restrict__ w1,
    const __hip_bfloat16* __restrict__ b1, const __hip_bfloat16* __restrict__ w2,
    const __hip_bfloat16* __restrict__ b2, __hip_bfloat16* __restrict__ out,
    unsigned long long* __restrict__ wsp)
{
    __shared__ float s_xn[D], s_y[D];
    __shared__ float s_red[25 * 160];
    __shared__ float s_q[DH], s_h[64], s_o[DH], s_ml[16], s_w[8];

    const int b = blockIdx.x, grp = b >> 3, c = b & 7;
    const int tid = threadIdx.x, lane = tid & 63, wave = tid >> 6;
    const bool mlp = grp < 27;
    gu64* ws = (gu64*)wsp;

    unsigned short xr[10];
#pragma unroll
    for (int i = 0; i < 10; ++i) xr[i] = ((const unsigned short*)x)[lane + 64 * i];
    const unsigned ep_old = __hip_atomic_load((gu32*)ws, RLX_AGENT);

    v4u rq[5], r1[3], rk[8], rv[8], ro[6], r2[3];
    {
        const int r16 = tid >> 4, s16 = tid & 15;
#pragma unroll
        for (int j = 0; j < 5; ++j)
            rq[j] = *(const v4u*)(wq + (size_t)(80 * c + r16 + 16 * j) * (H * DH) + 128 * grp + 8 * s16);
        if (mlp) {
            const int r8 = tid >> 3, s8 = tid & 7;
#pragma unroll
            for (int j = 0; j < 3; ++j) {
                const int r = r8 + 32 * j;
                r1[j] = (r < 80) ? *(const v4u*)(w1 + (size_t)(80 * c + r) * F + 64 * grp + 8 * s8) : v4u{0, 0, 0, 0};
            }
        } else {
#pragma unroll
            for (int j = 0; j < 3; ++j) r1[j] = v4u{0, 0, 0, 0};
        }
        const size_t kbase = ((size_t)grp * SK + (SK - WIN) + 128 * c + r16) * DH + 8 * s16;
#pragma unroll
        for (int j = 0; j < 8; ++j) rk[j] = *(const v4u*)(kc + kbase + (size_t)16 * j * DH);
#pragma unroll
        for (int j = 0; j < 8; ++j) rv[j] = *(const v4u*)(vc + kbase + (size_t)16 * j * DH);
        const int r10 = tid / 10, s10 = tid - r10 * 10;
        const bool act = tid < 250;
#pragma unroll
        for (int j = 0; j < 6; ++j) {
            const int r = r10 + 25 * j;
            ro[j] = (act && r < 128) ? *(const v4u*)(wo + (size_t)(128 * grp + r) * D + 80 * c + 8 * s10) : v4u{0, 0, 0, 0};
        }
#pragma unroll
        for (int j = 0; j < 3; ++j) {
            const int r = r10 + 25 * j;
            r2[j] = (mlp && act && r < 64) ? *(const v4u*)(w2 + (size_t)(64 * grp + r) * D + 80 * c + 8 * s10) : v4u{0, 0, 0, 0};
        }
    }

    {
        float xv[10], s = 0.f;
#pragma unroll
        for (int i = 0; i < 10; ++i) { xv[i] = __uint_as_float((unsigned)xr[i] << 16); s += xv[i]; }
        const float m = wsum(s) * (1.f / D);
        float v = 0.f;
#pragma unroll
        for (int i = 0; i < 10; ++i) { const float d = xv[i] - m; v += d * d; }
        v = wsum(v) * (1.f / D);
        const float rs = 1.f / sqrtf(v + 1e-5f);
        s = 0.f;
#pragma unroll
        for (int i = 0; i < 10; ++i) { xv[i] = rbf((xv[i] - m) * rs); s += xv[i]; }
        const float m2 = wsum(s) * (1.f / D);
        float v2 = 0.f;
#pragma unroll
        for (int i = 0; i < 10; ++i) { const float d = xv[i] - m2; v2 += d * d; }
        v2 = wsum(v2) * (1.f / D);
        const float rs2 = 1.f / sqrtf(v2 + 1e-5f);
        if (wave == 0) {
#pragma unroll
            for (int i = 0; i < 10; ++i) { s_xn[lane + 64 * i] = xv[i]; s_y[lane + 64 * i] = rbf((xv[i] - m2) * rs2); }
        }
    }
    __syncthreads();
    const unsigned tag = ep_old + 1u;

    {
        float aq[8] = {0, 0, 0, 0, 0, 0, 0, 0}, ah[8] = {0, 0, 0, 0, 0, 0, 0, 0};
        const int r16 = tid >> 4;
#pragma unroll
        for (int j = 0; j < 5; ++j) {
            const float xs = s_xn[80 * c + r16 + 16 * j];
            float w[8]; unpack8(rq[j], w);
#pragma unroll
            for (int i = 0; i < 8; ++i) aq[i] += xs * w[i];
        }
        const int r8 = tid >> 3;
#pragma unroll
        for (int j = 0; j < 3; ++j) {
            const int r = r8 + 32 * j;
            const float ys = (r < 80) ? s_y[80 * c + r] : 0.f;
            float w[8]; unpack8(r1[j], w);
#pragma unroll
            for (int i = 0; i < 8; ++i) ah[i] += ys * w[i];
        }
#pragma unroll
        for (int i = 0; i < 8; ++i) {
            aq[i] += __shfl_xor(aq[i], 16); aq[i] += __shfl_xor(aq[i], 32);
            ah[i] += __shfl_xor(ah[i], 8); ah[i] += __shfl_xor(ah[i], 16); ah[i] += __shfl_xor(ah[i], 32);
        }
        if (lane < 16) {
#pragma unroll
            for (int i = 0; i < 8; ++i) s_red[wave * 192 + 8 * lane + i] = aq[i];
        }
        if (lane < 8) {
#pragma unroll
            for (int i = 0; i < 8; ++i) s_red[wave * 192 + 128 + 8 * lane + i] = ah[i];
        }
    }
    __syncthreads();
    const int n1 = mlp ? 192 : 128;
    if (tid < n1) {
        const float v = s_red[tid] + s_red[192 + tid] + s_red[384 + tid] + s_red[576 + tid];
        put(ws + OFF_X1 + (size_t)b * X1N + tid, tag, v);
    }
    if (tid < n1) {
        float pv[8];
        sweep<8>(ws + OFF_X1 + (size_t)(grp * 8) * X1N + tid, X1N, tag, pv);
        float s = 0.f;
#pragma unroll
        for (int k = 0; k < 8; ++k) s += pv[k];
        s = rbf(s);
        if (tid < 128) s_q[tid] = s;
        else {
            const int col = 64 * grp + tid - 128;
            float hv = rbf(s + __bfloat162float(b1[col]));
            const float u = 0.7978845608028654f * (hv + 0.044715f * hv * hv * hv);
            s_h[tid - 128] = rbf(0.5f * hv * (1.f + tanhf(u)));
        }
    }
    __syncthreads();

    {
        float qf[8];
#pragma unroll
        for (int i = 0; i < 8; ++i) qf[i] = s_q[8 * (tid & 15) + i];
        float sc[8];
#pragma unroll
        for (int j = 0; j < 8; ++j) {
            float kf[8]; unpack8(rk[j], kf);
            float d = 0.f;
#pragma unroll
            for (int i = 0; i < 8; ++i) d += qf[i] * kf[i];
            d += __shfl_xor(d, 1); d += __shfl_xor(d, 2); d += __shfl_xor(d, 4); d += __shfl_xor(d, 8);
            sc[j] = rbf(rbf(d) * 0.08838834764831845f);
        }
        float mx = sc[0];
#pragma unroll
        for (int j = 1; j < 8; ++j) mx = fmaxf(mx, sc[j]);
        mx = fmaxf(mx, __shfl_xor(mx, 16)); mx = fmaxf(mx, __shfl_xor(mx, 32));
        if (lane == 0) s_w[wave] = mx;
        __syncthreads();
        mx = fmaxf(fmaxf(s_w[0], s_w[1]), fmaxf(s_w[2], s_w[3]));
        float l = 0.f, ao[8] = {0, 0, 0, 0, 0, 0, 0, 0};
#pragma unroll
        for (int j = 0; j < 8; ++j) {
            const float p = __expf(sc[j] - mx);
            l += p;
            float vf[8]; unpack8(rv[j], vf);
#pragma unroll
            for (int i = 0; i < 8; ++i) ao[i] += p * vf[i];
        }
        l += __shfl_xor(l, 16); l += __shfl_xor(l, 32);
#pragma unroll
        for (int i = 0; i < 8; ++i) { ao[i] += __shfl_xor(ao[i], 16); ao[i] += __shfl_xor(ao[i], 32); }
        if (lane < 16) {
#pragma unroll
            for (int i = 0; i < 8; ++i) s_red[wave * 136 + 8 * lane + i] = ao[i];
        }
        if (lane == 0) { s_red[wave * 136 + 128] = 0.f; s_red[wave * 136 + 129] = l; }
        __syncthreads();
        if (tid < X2N) {
            float v = s_red[tid] + s_red[136 + tid] + s_red[272 + tid] + s_red[408 + tid];
            if (tid == 128) v = mx;
            put(ws + OFF_X2 + (size_t)b * 136 + tid, tag, v);
        }
    }
    {
        float pv[8];
        if (tid < X2N) {
            sweep<8>(ws + OFF_X2 + (size_t)(grp * 8) * 136 + tid, 136, tag, pv);
            if (tid >= 128) {
#pragma unroll
                for (int k = 0; k < 8; ++k) s_ml[(tid - 128) * 8 + k] = pv[k];
            }
        }
        __syncthreads();
        if (tid < 128) {
            float M = s_ml[0];
#pragma unroll
            for (int k = 1; k < 8; ++k) M = fmaxf(M, s_ml[k]);
            float L = 0.f, o = 0.f;
#pragma unroll
            for (int k = 0; k < 8; ++k) { const float e = __expf(s_ml[k] - M); L += e * s_ml[8 + k]; o += e * pv[k]; }
            s_o[tid] = rbf(o / L);
        }
        __syncthreads();
    }
    {
        const int r10 = tid / 10, s10 = tid - r10 * 10;
        float aa[8] = {0, 0, 0, 0, 0, 0, 0, 0}, ab[8] = {0, 0, 0, 0, 0, 0, 0, 0};
        if (tid < 250) {
#pragma unroll
            for (int j = 0; j < 6; ++j) {
                const int r = r10 + 25 * j;
                const float os = (r < 128) ? s_o[r] : 0.f;
                float w[8]; unpack8(ro[j], w);
#pragma unroll
                for (int i = 0; i < 8; ++i) aa[i] += os * w[i];
            }
#pragma unroll
            for (int j = 0; j < 3; ++j) {
                const int r = r10 + 25 * j;
                const float hs = (mlp && r < 64) ? s_h[r] : 0.f;
                float w[8]; unpack8(r2[j], w);
#pragma unroll
                for (int i = 0; i < 8; ++i) ab[i] += hs * w[i];
            }
#pragma unroll
            for (int i = 0; i < 8; ++i) { s_red[r10 * 160 + 8 * s10 + i] = aa[i]; s_red[r10 * 160 + 80 + 8 * s10 + i] = ab[i]; }
        }
        __syncthreads();
        if (tid < X3N) {
            float v = 0.f;
#pragma unroll
            for (int k = 0; k < 25; ++k) v += s_red[k * 160 + tid];
            put(ws + OFF_X3 + ((size_t)c * 32 + grp) * X3N + tid, tag, v);
        }
    }
    if (grp == 31) {
        __syncthreads();
        if (tid < X3N) {
            float pv[32];
            sweep<32>(ws + OFF_X3 + (size_t)c * 32 * X3N + tid, X3N, tag, pv);
            float s = 0.f;
#pragma unroll
            for (int k = 0; k < 32; ++k) s += pv[k];
            s_red[tid] = rbf(s);
        }
        __syncthreads();
        if (tid < 80) {
            const int col = 80 * c + tid;
            const float xa = rbf(__bfloat162float(x[col]) + s_red[tid]);
            const float bb = rbf(s_red[80 + tid] + __bfloat162float(b2[col]));
            out[col] = __float2bfloat16(xa + bb);
        }
        if (c == 0 && tid == 0) __hip_atomic_store((gu32*)ws, tag, RLX_AGENT);
    }
}

extern "C" void kernel_launch(void* const* d_in, const int* in_sizes, int n_in,
                              void* d_out, int out_size, void* d_ws, size_t ws_size,
                              hipStream_t stream) {
    const __hip_bfloat16* x = (const __hip_bfloat16*)d_in[0];
    const __hip_bfloat16* kc = (const __hip_bfloat16*)d_in[1];
    const __hip_bfloat16* vc = (const __hip_bfloat16*)d_in[2];
    const __hip_bfloat16* wq = (const __hip_bfloat16*)d_in[3];
    const __hip_bfloat16* wo = (const __hip_bfloat16*)d_in[4];
    const __hip_bfloat16* w1 = (const __hip_bfloat16*)d_in[6];
    const __hip_bfloat16* b1 = (const __hip_bfloat16*)d_in[7];
    const __hip_bfloat16* w2 = (const __hip_bfloat16*)d_in[8];
    const __hip_bfloat16* b2 = (const __hip_bfloat16*)d_in[9];
    fused_kernel<<<256, 256, 0, stream>>>(x, kc, vc, wq, wo, w1, b1, w2, b2,
                                          (__hip_bfloat16*)d_out, (unsigned long long*)d_ws);
}
```

```cpp
#include <hip/hip_runtime.h>
#include <hip/hip_bf16.h>

#define GAS __attribute__((address_space(1)))
typedef unsigned v4u __attribute__((ext_vector_type(4)));
typedef GAS unsigned gu32;
typedef GAS unsigned long long gu64;
#define RLX_AGENT __ATOMIC_RELAXED, __HIP_MEMORY_SCOPE_AGENT

constexpr int D = 640, H = 32, DH = 128, SK = 4096, WIN = 1024, F = 1728;
constexpr int X1N = 192, X2N = 130, X3N = 160;
constexpr int OFF_X1 = 32;
constexpr int OFF_X2 = OFF_X1 + 256 * X1N;
constexpr int OFF_X3 = OFF_X2 + 256 * 136;
constexpr unsigned SPIN_CAP = 1u << 22;

__device__ __forceinline__ float blo(unsigned u) { return __uint_as_float(u << 16); }
__device__ __forceinline__ float bhi(unsigned u) { return __uint_as_float(u & 0xffff0000u); }
__device__ __forceinline__ float rbf(float f) { return __bfloat162float(__float2bfloat16(f)); }
__device__ __forceinline__ void unpack8(const v4u& u, float (&f)[8]) {
    f[0] = blo(u.x); f[1] = bhi(u.x); f[2] = blo(u.y); f[3] = bhi(u.y);
    f[4] = blo(u.z); f[5] = bhi(u.z); f[6] = blo(u.w); f[7] = bhi(u.w);
}
template <int C> __device__ __forceinline__ float dpp(float v) {
    return __int_as_float(__builtin_amdgcn_update_dpp(0, __float_as_int(v), C, 0xf, 0xf, true));
}
__device__ __forceinline__ float sum16(float v) {
    v += dpp<0xB1>(v); v += dpp<0x4E>(v); v += dpp<0x141>(v); v += dpp<0x140>(v);
    return v;
}
__device__ __forceinline__ float rdl(float v, int l) { return __int_as_float(__builtin_amdgcn_readlane(__float_as_int(v), l)); }
__device__ __forceinline__ float wsum(float v) {
    v = sum16(v);
    return (rdl(v, 0) + rdl(v, 16)) + (rdl(v, 32) + rdl(v, 48));
}
__device__ __forceinline__ void put(gu64* g, unsigned tag, float v) {
    __hip_atomic_store(g, ((unsigned long long)tag << 32) | __float_as_uint(v), RLX_AGENT);
}
template <int N>
__device__ __forceinline__ void sweep(gu64* g, int S, unsigned tag, float (&v)[N]) {
    for (unsigned spins = 0; spins < SPIN_CAP; ++spins) {
        bool ok = true;
#pragma unroll
        for (int k = 0; k < N; ++k) {
            const unsigned long long q = __hip_atomic_load(g + (size_t)k * S, RLX_AGENT);
            v[k] = __uint_as_float((unsigned)q);
            ok &= (unsigned)(q >> 32) == tag;
        }
        if (ok) break;
    }
}

#define STAMP(i)
__global__ __launch_bounds__(256) void fused_kernel(
    const __hip_bfloat16* __restrict__ x, const __hip_bfloat16* __restrict__ kc,
    const __hip_bfloat16* __restrict__ vc, const __hip_bfloat16* __restrict__ wq,
    const __hip_bfloat16* __restrict__ wo, const __hip_bfloat16* __restrict__ w1,
    const __hip_bfloat16* __restrict__ b1, const __hip_bfloat16* __restrict__ w2,
    const __hip_bfloat16* __restrict__ b2, __hip_bfloat16* __restrict__ out,
    unsigned long long* __restrict__ wsp)
{
    __shared__ float s_xn[D], s_y[D];
    __shared__ float s_red[4096];
    __shared__ float s_q[DH], s_h[64], s_o[DH], s_ml[16], s_w[16], s_l[16];

    const int b = blockIdx.x, grp = b >> 3, c = b & 7;
    const int tid = threadIdx.x, lane = tid & 63, wave = tid >> 6;
    const bool mlp = grp < 27;
    gu64* ws = (gu64*)wsp;

    STAMP(0);
    unsigned short xr[10];
#pragma unroll
    for (int i = 0; i < 10; ++i) xr[i] = ((const unsigned short*)x)[lane + 64 * i];
    const unsigned ep_old = __hip_atomic_load((gu32*)ws, RLX_AGENT);

    v4u rq[5], r1[3], rk[8], rv[8], ro[6], r2[3];
    {
        const int r16 = tid >> 4, s16 = tid & 15;
#pragma unroll
        for (int j = 0; j < 5; ++j)
            rq[j] = *(const v4u*)(wq + (size_t)(80 * c + r16 + 16 * j) * (H * DH) + 128 * grp + 8 * s16);
        {
            const int r8 = tid >> 3, s8 = tid & 7, g1 = mlp ? grp : 0;
#pragma unroll
            for (int j = 0; j < 3; ++j) {
                const int r = min(r8 + 32 * j, 79);
                r1[j] = *(const v4u*)(w1 + (size_t)(80 * c + r) * F + 64 * g1 + 8 * s8);
            }
        }
        const size_t kbase = ((size_t)grp * SK + (SK - WIN) + 128 * c + r16) * DH + 8 * s16;
#pragma unroll
        for (int j = 0; j < 8; ++j) rk[j] = *(const v4u*)(kc + kbase + (size_t)16 * j * DH);
#pragma unroll
        for (int j = 0; j < 8; ++j) rv[j] = *(const v4u*)(vc + kbase + (size_t)16 * j * DH);
        const int r10 = tid / 10, s10 = tid - r10 * 10;
#pragma unroll
        for (int j = 0; j < 6; ++j) {
            const int r = min(r10 + 25 * j, 127);
            ro[j] = *(const v4u*)(wo + (size_t)(128 * grp + r) * D + 80 * c + 8 * s10);
        }
#pragma unroll
        for (int j = 0; j < 3; ++j) {
            const int r = min(r10 + 25 * j, 63);
            r2[j] = *(const v4u*)(w2 + (size_t)(64 * (mlp ? grp : 0) + r) * D + 80 * c + 8 * s10);
        }
    }

    __builtin_amdgcn_sched_barrier(0);
    {
        float xv[10], s = 0.f;
#pragma unroll
        for (int i = 0; i < 10; ++i) { xv[i] = __uint_as_float((unsigned)xr[i] << 16); s += xv[i]; }
        const float m = wsum(s) * (1.f / D);
        float v = 0.f;
#pragma unroll
        for (int i = 0; i < 10; ++i) { const float d = xv[i] - m; v += d * d; }
        v = wsum(v) * (1.f / D);
        const float rs = 1.f / sqrtf(v + 1e-5f);
        s = 0.f;
#pragma unroll
        for (int i = 0; i < 10; ++i) { xv[i] = rbf((xv[i] - m) * rs); s += xv[i]; }
        const float m2 = wsum(s) * (1.f / D);
        float v2 = 0.f;
#pragma unroll
        for (int i = 0; i < 10; ++i) { const float d = xv[i] - m2; v2 += d * d; }
        v2 = wsum(v2) * (1.f / D);
        const float rs2 = 1.f / sqrtf(v2 + 1e-5f);
        if (wave == 0) {
#pragma unroll
            for (int i = 0; i < 10; ++i) { s_xn[lane + 64 * i] = xv[i]; s_y[lane + 64 * i] = rbf((xv[i] - m2) * rs2); }
        }
    }
    __syncthreads();
    const unsigned tag = ep_old + 1u;
    STAMP(1);

    {
        float aq[8] = {0, 0, 0, 0, 0, 0, 0, 0}, ah[8] = {0, 0, 0, 0, 0, 0, 0, 0};
        const int r16 = tid >> 4;
#pragma unroll
        for (int j = 0; j < 5; ++j) {
            const float xs = s_xn[80 * c + r16 + 16 * j];
            float w[8]; unpack8(rq[j], w);
#pragma unroll
            for (int i = 0; i < 8; ++i) aq[i] += xs * w[i];
        }
        const int r8 = tid >> 3;
#pragma unroll
        for (int j = 0; j < 3; ++j) {
            const int r = r8 + 32 * j;
            const float ys = (r < 80) ? s_y[80 * c + r] : 0.f;
            float w[8]; unpack8(r1[j], w);
#pragma unroll
            for (int i = 0; i < 8; ++i) ah[i] += ys * w[i];
        }
#pragma unroll
        for (int i = 0; i < 8; ++i) { s_red[8 * tid + i] = aq[i]; s_red[2048 + 8 * tid + i] = ah[i]; }
    }
    __syncthreads();
    const int n1 = mlp ? 192 : 128;
    if (tid < n1) {
        float v = 0.f;
        if (tid < 128) {
#pragma unroll
            for (int k = 0; k < 16; ++k) v += s_red[k * 128 + tid];
        } else {
#pragma unroll
            for (int k = 0; k < 32; ++k) v += s_red[2048 + k * 64 + tid - 128];
        }
        put(ws + OFF_X1 + (size_t)b * X1N + tid, tag, v);
    }
    STAMP(2);
    if (tid < n1) {
        float pv[8];
        sweep<8>(ws + OFF_X1 + (size_t)(grp * 8) * X1N + tid, X1N, tag, pv);
        float s = 0.f;
#pragma unroll
        for (int k = 0; k < 8; ++k) s += pv[k];
        s = rbf(s);
        if (tid < 128) s_q[tid] = s;
        else {
            const int col = 64 * grp + tid - 128;
            float hv = rbf(s + __bfloat162float(b1[col]));
            const float u = 0.7978845608028654f * (hv + 0.044715f * hv * hv * hv);
            s_h[tid - 128] = rbf(0.5f * hv * (1.f + tanhf(u)));
        }
    }
    __syncthreads();

    STAMP(3);
    {
        float qf[8];
#pragma unroll
        for (int i = 0; i < 8; ++i) qf[i] = s_q[8 * (tid & 15) + i];
        float sc[8];
#pragma unroll
        for (int j = 0; j < 8; ++j) {
            float kf[8]; unpack8(rk[j], kf);
            float d = 0.f;
#pragma unroll
            for (int i = 0; i < 8; ++i) d += qf[i] * kf[i];
            d = sum16(d);
            sc[j] = rbf(rbf(d) * 0.08838834764831845f);
        }
        float mx = sc[0];
#pragma unroll
        for (int j = 1; j < 8; ++j) mx = fmaxf(mx, sc[j]);
        if ((tid & 15) == 0) s_w[tid >> 4] = mx;
        __syncthreads();
#pragma unroll
        for (int k = 0; k < 16; ++k) mx = fmaxf(mx, s_w[k]);
        float l = 0.f, ao[8] = {0, 0, 0, 0, 0, 0, 0, 0};
#pragma unroll
        for (int j = 0; j < 8; ++j) {
            const float p = __expf(sc[j] - mx);
            l += p;
            float vf[8]; unpack8(rv[j], vf);
#pragma unroll
            for (int i = 0; i < 8; ++i) ao[i] += p * vf[i];
        }
#pragma unroll
        for (int i = 0; i < 8; ++i) s_red[8 * tid + i] = ao[i];
        if ((tid & 15) == 0) s_l[tid >> 4] = l;
        __syncthreads();
        if (tid < X2N) {
            float v = 0.f;
#pragma unroll
            for (int k = 0; k < 16; ++k) v += (tid < 128) ? s_red[k * 128 + tid] : s_l[k];
            if (tid == 128) v = mx;
            put(ws + OFF_X2 + (size_t)b * 136 + tid, tag, v);
        }
    }
    STAMP(4);
    {
        float pv[8];
        if (tid < X2N) {
            sweep<8>(ws + OFF_X2 + (size_t)(grp * 8) * 136 + tid, 136, tag, pv);
            if (tid >= 128) {
#pragma unroll
                for (int k = 0; k < 8; ++k) s_ml[(tid - 128) * 8 + k] = pv[k];
            }
        }
        __syncthreads();
        if (tid < 128) {
            float M = s_ml[0];
#pragma unroll
            for (int k = 1; k < 8; ++k) M = fmaxf(M, s_ml[k]);
            float L = 0.f, o = 0.f;
#pragma unroll
            for (int k = 0; k < 8; ++k) { const float e = __expf(s_ml[k] - M); L += e * s_ml[8 + k]; o += e * pv[k]; }
            s_o[tid] = rbf(o / L);
        }
        __syncthreads();
    }
    STAMP(5);
    {
        const int r10 = tid / 10, s10 = tid - r10 * 10;
        float aa[8] = {0, 0, 0, 0, 0, 0, 0, 0}, ab[8] = {0, 0, 0, 0, 0, 0, 0, 0};
        if (tid < 250) {
#pragma unroll
            for (int j = 0; j < 6; ++j) {
                const int r = r10 + 25 * j;
                const float os = (r < 128) ? s_o[r] : 0.f;
                float w[8]; unpack8(ro[j], w);
#pragma unroll
                for (int i = 0; i < 8; ++i) aa[i] += os * w[i];
            }
#pragma unroll
            for (int j = 0; j < 3; ++j) {
                const int r = r10 + 25 * j;
                const float hs = (mlp && r < 64) ? s_h[r] : 0.f;
                float w[8]; unpack8(r2[j], w);
#pragma unroll
                for (int i = 0; i < 8; ++i) ab[i] += hs * w[i];
            }
#pragma unroll
            for (int i = 0; i < 8; ++i) { s_red[r10 * 160 + 8 * s10 + i] = aa[i]; s_red[r10 * 160 + 80 + 8 * s10 + i] = ab[i]; }
        }
        __syncthreads();
        if (tid < X3N) {
            float v = 0.f;
#pragma unroll
            for (int k = 0; k < 25; ++k) v += s_red[k * 160 + tid];
            put(ws + OFF_X3 + ((size_t)c * 32 + grp) * X3N + tid, tag, v);
        }
    }
    STAMP(6);
    if (grp == 31) {
        __syncthreads();
        if (tid < X3N) {
            float pv[32];
            sweep<32>(ws + OFF_X3 + (size_t)c * 32 * X3N + tid, X3N, tag, pv);
            float s = 0.f;
#pragma unroll
            for (int k = 0; k < 32; ++k) s += pv[k];
            s_red[tid] = rbf(s);
        }
        __syncthreads();
        if (tid < 80) {
            const int col = 80 * c + tid;
            const float xa = rbf(__bfloat162float(x[col]) + s_red[tid]);
            const float bb = rbf(s_red[80 + tid] + __bfloat162float(b2[col]));
            out[col] = __float2bfloat16(xa + bb);
        }
        if (c == 0 && tid == 0) __hip_atomic_store((gu32*)ws, tag, RLX_AGENT);
        STAMP(7);
    }
}

extern "C" void kernel_launch(void* const* d_in, const int* in_sizes, int n_in,
                              void* d_out, int out_size, void* d_ws, size_t ws_size,
                              hipStream_t stream) {
    const __hip_bfloat16* x = (const __hip_bfloat16*)d_in[0];
    const __hip_bfloat16* kc = (const __hip_bfloat16*)d_in[1];
    const __hip_bfloat16* vc = (const __hip_bfloat16*)d_in[2];
    const __hip_bfloat16* wq = (const __hip_bfloat16*)d_in[3];
    const __hip_bfloat16* wo = (const __hip_bfloat16*)d_in[4];
    const __hip_bfloat16* w1 = (const __hip_bfloat16*)d_in[6];
    const __hip_bfloat16* b1 = (const __hip_bfloat16*)d_in[7];
    const __hip_bfloat16* w2 = (const __hip_bfloat16*)d_in[8];
    const __hip_bfloat16* b2 = (const __hip_bfloat16*)d_in[9];
    fused_kernel<<<256, 256, 0, stream>>>(x, kc, vc, wq, wo, w1, b1, w2, b2,
                                          (__hip_bfloat16*)d_out, (unsigned long long*)d_ws);
}
```
